```python
import torch

def _softmax(x, axis=-1):
    return torch.softmax(x, dim=axis)
def _gelu(x):
    return torch.nn.functional.gelu(x, approximate="tanh")
def _silu(x):
    return torch.nn.functional.silu(x)
def _sigmoid(x):
    return torch.sigmoid(x)
def _layernorm(x, axis=-1, eps=1e-5):
    m = x.mean(dim=axis, keepdim=True)
    v = x.var(dim=axis, keepdim=True, unbiased=False)
    return (x - m) / torch.sqrt(v + eps)
def _scatter_add(tgt, idx, src, axis=0):
    return tgt.clone().index_add_(axis, idx, src)
def _segment_sum(src, seg, n):
    out = torch.zeros((n,) + src.shape[1:], dtype=src.dtype)
    return out.index_add_(0, seg, src)
def _pad(x, pw):
    flat = tuple(v for pair in reversed(pw) for v in pair)
    return torch.nn.functional.pad(x, flat)

kernel_name = 'standalone__low_rank_proj_linformer__decode'

def setup_inputs():

    _w = (lambda a: (lambda t: t.to(torch.bfloat16) if t.is_floating_point() else t)(torch.as_tensor(a)))
    B, H, Sq, Skv, Dh, R = 2, 32, 1, 8192, 128, 64
    return {
        "q": _w((lambda *s: torch.randn(*s, dtype=torch.bfloat16) * 0.8)(B, H, Sq, Dh)),
        "k": _w((lambda *s: torch.randn(*s, dtype=torch.bfloat16) * 0.8)(B, H, Skv, Dh)),
        "v": _w((lambda *s: torch.randn(*s, dtype=torch.bfloat16) * 0.05)(B, H, Skv, Dh)),
        "e_proj": _w((lambda *s: torch.randn(*s, dtype=torch.bfloat16) * 0.8)(R, Skv) * (Skv ** -0.5)),
        "f_proj": _w((lambda *s: torch.randn(*s, dtype=torch.bfloat16) * 0.05)(R, Skv) * (Skv ** -0.5)),
    }

def reference(q, k, v, e_proj, f_proj):
    B, H, Sq, Dh = q.shape
    kp = torch.einsum("rn,bhnd->bhrd", e_proj, k)
    vp = torch.einsum("rn,bhnd->bhrd", f_proj, v)
    scores = torch.einsum("bhqd,bhrd->bhqr", q, kp) * (Dh ** -0.5)
    p = _softmax(scores, axis=-1)
    o = torch.einsum("bhqr,bhrd->bhqd", p, vp)
    return o
```

```cpp
#include <hip/hip_runtime.h>
#include <hip/hip_bf16.h>
#include <stdint.h>

typedef unsigned int u32;
typedef unsigned short u16;
typedef __attribute__((ext_vector_type(4))) unsigned int u32x4;
typedef __attribute__((ext_vector_type(2))) __bf16 bf16x2;

constexpr int DH = 128;
constexpr int RK = 64;
constexpr int CHUNK = 512;

__device__ __forceinline__ float bf_lo(u32 x) { return __builtin_bit_cast(float, x << 16); }
__device__ __forceinline__ float bf_hi(u32 x) { return __builtin_bit_cast(float, x & 0xffff0000u); }
__device__ __forceinline__ float dot2(u32 a, u32 b, float acc) {
  return __builtin_amdgcn_fdot2_f32_bf16(__builtin_bit_cast(bf16x2, a), __builtin_bit_cast(bf16x2, b), acc, false);
}

__global__ void __launch_bounds__(256) k1_scores(const u16* __restrict__ q, const u16* __restrict__ k,
                                                 const u16* __restrict__ e, float* __restrict__ part_sc,
                                                 int N, int NC) {
  __shared__ float s_lds[CHUNK];
  __shared__ float red[4][RK];
  const int c = blockIdx.x, h = blockIdx.y;
  const int tid = threadIdx.x, lane = tid & 63, wave = tid >> 6;
  const int g = lane >> 4, cc = lane & 15;
  const u16* kp = k + ((size_t)h * N + (size_t)c * CHUNK) * DH;
  const u32x4 qv = *reinterpret_cast<const u32x4*>(q + (size_t)h * DH + cc * 8);
  const int row0 = wave * (CHUNK / 4);
#pragma unroll 8
  for (int i = 0; i < CHUNK / 16; ++i) {
    const int row = row0 + 4 * i + g;
    const u32x4 kv = __builtin_nontemporal_load(reinterpret_cast<const u32x4*>(kp + (size_t)row * DH + cc * 8));
    float d = 0.f;
    d = dot2(kv[0], qv[0], d); d = dot2(kv[1], qv[1], d); d = dot2(kv[2], qv[2], d); d = dot2(kv[3], qv[3], d);
    d += __shfl_xor(d, 1); d += __shfl_xor(d, 2); d += __shfl_xor(d, 4); d += __shfl_xor(d, 8);
    if (cc == 0) s_lds[row] = d;
  }
  __syncthreads();
  {
    const int r = tid & 63, qq = tid >> 6;
    const u16* ep = e + (size_t)r * N + (size_t)c * CHUNK + qq * (CHUNK / 4);
    const float* sp = s_lds + qq * (CHUNK / 4);
    float acc = 0.f;
#pragma unroll 4
    for (int j = 0; j < CHUNK / 4; j += 8) {
      const u32x4 ev = *reinterpret_cast<const u32x4*>(ep + j);
      acc = fmaf(bf_lo(ev[0]), sp[j + 0], acc); acc = fmaf(bf_hi(ev[0]), sp[j + 1], acc);
      acc = fmaf(bf_lo(ev[1]), sp[j + 2], acc); acc = fmaf(bf_hi(ev[1]), sp[j + 3], acc);
      acc = fmaf(bf_lo(ev[2]), sp[j + 4], acc); acc = fmaf(bf_hi(ev[2]), sp[j + 5], acc);
      acc = fmaf(bf_lo(ev[3]), sp[j + 6], acc); acc = fmaf(bf_hi(ev[3]), sp[j + 7], acc);
    }
    red[qq][r] = acc;
  }
  __syncthreads();
  if (tid < RK) {
    const float t = (red[0][tid] + red[1][tid]) + (red[2][tid] + red[3][tid]);
    part_sc[((size_t)h * NC + c) * RK + tid] = t;
  }
}

__global__ void __launch_bounds__(256) k2_out(const u16* __restrict__ v, const u16* __restrict__ f,
                                              const float* __restrict__ part_sc, float* __restrict__ part_o,
                                              int N, int NC, float scale) {
  __shared__ float p_lds[RK];
  __shared__ float w_lds[CHUNK];
  __shared__ float red[4][DH];
  const int c = blockIdx.x, h = blockIdx.y;
  const int tid = threadIdx.x, lane = tid & 63, wave = tid >> 6;
  const int g = lane >> 4, cc = lane & 15;
  if (wave == 0) {
    float sc = 0.f;
    for (int i = 0; i < NC; ++i) sc += part_sc[((size_t)h * NC + i) * RK + lane];
    sc *= scale;
    float m = sc;
    for (int o = 32; o >= 1; o >>= 1) m = fmaxf(m, __shfl_xor(m, o));
    const float ex = __expf(sc - m);
    float sum = ex;
    for (int o = 32; o >= 1; o >>= 1) sum += __shfl_xor(sum, o);
    p_lds[lane] = ex / sum;
  }
  __syncthreads();
  {
    const u16* fp = f + (size_t)c * CHUNK + tid * 2;
    float a0 = 0.f, a1 = 0.f;
#pragma unroll 8
    for (int r = 0; r < RK; ++r) {
      const u32 fv = *reinterpret_cast<const u32*>(fp + (size_t)r * N);
      const float pr = p_lds[r];
      a0 = fmaf(pr, bf_lo(fv), a0); a1 = fmaf(pr, bf_hi(fv), a1);
    }
    w_lds[tid * 2] = a0; w_lds[tid * 2 + 1] = a1;
  }
  __syncthreads();
  const u16* vp = v + ((size_t)h * N + (size_t)c * CHUNK) * DH;
  float acc[8];
#pragma unroll
  for (int j = 0; j < 8; ++j) acc[j] = 0.f;
  const int row0 = wave * (CHUNK / 4);
#pragma unroll 8
  for (int i = 0; i < CHUNK / 16; ++i) {
    const int row = row0 + 4 * i + g;
    const u32x4 vv = __builtin_nontemporal_load(reinterpret_cast<const u32x4*>(vp + (size_t)row * DH + cc * 8));
    const float w = w_lds[row];
    acc[0] = fmaf(w, bf_lo(vv[0]), acc[0]); acc[1] = fmaf(w, bf_hi(vv[0]), acc[1]);
    acc[2] = fmaf(w, bf_lo(vv[1]), acc[2]); acc[3] = fmaf(w, bf_hi(vv[1]), acc[3]);
    acc[4] = fmaf(w, bf_lo(vv[2]), acc[4]); acc[5] = fmaf(w, bf_hi(vv[2]), acc[5]);
    acc[6] = fmaf(w, bf_lo(vv[3]), acc[6]); acc[7] = fmaf(w, bf_hi(vv[3]), acc[7]);
  }
#pragma unroll
  for (int j = 0; j < 8; ++j) { acc[j] += __shfl_xor(acc[j], 16); acc[j] += __shfl_xor(acc[j], 32); }
  if (g == 0) {
#pragma unroll
    for (int j = 0; j < 8; ++j) red[wave][cc * 8 + j] = acc[j];
  }
  __syncthreads();
  if (tid < DH) {
    const float t = (red[0][tid] + red[1][tid]) + (red[2][tid] + red[3][tid]);
    part_o[((size_t)h * NC + c) * DH + tid] = t;
  }
}

__global__ void __launch_bounds__(128) k3_final(const float* __restrict__ part_o, __hip_bfloat16* __restrict__ out, int NC) {
  const int h = blockIdx.x, d = threadIdx.x;
  float t = 0.f;
  for (int i = 0; i < NC; ++i) t += part_o[((size_t)h * NC + i) * DH + d];
  out[(size_t)h * DH + d] = __float2bfloat16(t);
}

extern "C" void kernel_launch(void* const* d_in, const int* in_sizes, int n_in,
                              void* d_out, int out_size, void* d_ws, size_t ws_size,
                              hipStream_t stream) {
  const u16* q = (const u16*)d_in[0];
  const u16* k = (const u16*)d_in[1];
  const u16* v = (const u16*)d_in[2];
  const u16* e = (const u16*)d_in[3];
  const u16* f = (const u16*)d_in[4];
  const int BH = out_size / DH;
  const int N = in_sizes[3] / RK;
  const int NC = N / CHUNK;
  float* part_sc = (float*)d_ws;
  float* part_o = (float*)((char*)d_ws + (size_t)BH * NC * RK * 4);
  const float scale = 1.0f / sqrtf((float)DH);
  dim3 grid(NC, BH);
  k1_scores<<<grid, 256, 0, stream>>>(q, k, e, part_sc, N, NC);
  k2_out<<<grid, 256, 0, stream>>>(v, f, part_sc, part_o, N, NC, scale);
  k3_final<<<BH, 128, 0, stream>>>(part_o, (__hip_bfloat16*)d_out, NC);
}
```

```cpp
#include <hip/hip_runtime.h>
#include <hip/hip_bf16.h>
#include <stdint.h>

typedef unsigned int u32;
typedef unsigned short u16;
typedef __attribute__((ext_vector_type(4))) unsigned int u32x4;
typedef __attribute__((ext_vector_type(4))) float f32x4;
typedef __attribute__((ext_vector_type(2))) float f32x2;
typedef __attribute__((ext_vector_type(2))) __bf16 bf16x2;

#define WAVES 4
#define TILES 4
#define DEPTH 8
constexpr int DH = 128;
constexpr int RK = 64;
constexpr int TROWS = 64;
constexpr int NSEQ = 8192;
constexpr int NBH = 64;
constexpr int RROWS = TILES * TROWS;
constexpr int NR = NSEQ / RROWS;
constexpr int NTHR = WAVES * 64;
constexpr int PSTRIDE = RROWS * 2 + 16;
constexpr int STAGE_VEC = RK * RROWS * 2 / 16;
constexpr int STAGE_PER_THR = STAGE_VEC / NTHR;
static_assert(STAGE_VEC % NTHR == 0, "staging");

__device__ __forceinline__ float bf_lo(u32 x) { return __builtin_bit_cast(float, x << 16); }
__device__ __forceinline__ float bf_hi(u32 x) { return __builtin_bit_cast(float, x & 0xffff0000u); }
__device__ __forceinline__ float dot2(u32 a, u32 b, float acc) {
  return __builtin_amdgcn_fdot2_f32_bf16(__builtin_bit_cast(bf16x2, a), __builtin_bit_cast(bf16x2, b), acc, false);
}
__device__ __forceinline__ u32x4 ld_nt(const u16* p) { return __builtin_nontemporal_load(reinterpret_cast<const u32x4*>(p)); }
__device__ __forceinline__ u32x4 ld_c(const u16* p) { return *reinterpret_cast<const u32x4*>(p); }
template <int CTRL> __device__ __forceinline__ float dpp(float x) {
  return __builtin_bit_cast(float, __builtin_amdgcn_mov_dpp(__builtin_bit_cast(int, x), CTRL, 0xf, 0xf, true));
}
constexpr int XOR1 = 0xB1, XOR2 = 0x4E, XOR7 = 0x141, XOR8 = 0x128;
__device__ __forceinline__ float swap32_sum(float a, float b) {
  auto r = __builtin_amdgcn_permlane32_swap(__float_as_uint(a), __float_as_uint(b), false, false);
  return __uint_as_float(r[0]) + __uint_as_float(r[1]);
}
__device__ __forceinline__ float swap16_sum(float a, float b) {
  auto r = __builtin_amdgcn_permlane16_swap(__float_as_uint(a), __float_as_uint(b), false, false);
  return __uint_as_float(r[0]) + __uint_as_float(r[1]);
}

__global__ void __launch_bounds__(NTHR) k1_scores(const u16* __restrict__ q, const u16* __restrict__ k,
                                                  const u16* __restrict__ e, float* __restrict__ part_sc) {
  constexpr int N = NSEQ;
  __shared__ __attribute__((aligned(16))) unsigned char e_lds[RK * PSTRIDE];
  __shared__ __attribute__((aligned(16))) float s_lds[WAVES][TROWS];
  const int nr = blockIdx.x;
  const int tid = threadIdx.x, lane = tid & 63, wave = __builtin_amdgcn_readfirstlane(tid >> 6);
  const int h = blockIdx.y * WAVES + wave;
  const int g = lane >> 4, c = lane & 15;
  const u16* kp = k + ((size_t)h * N + nr * RROWS + g) * DH + c * 8;
  const u32x4 qv = ld_c(q + (size_t)h * DH + c * 8);
  u32x4 kv[DEPTH];
#pragma unroll
  for (int v = 0; v < DEPTH; ++v) kv[v] = ld_nt(kp + (size_t)(4 * v) * DH);
  u32x4 st[STAGE_PER_THR];
#pragma unroll
  for (int i = 0; i < STAGE_PER_THR; ++i) {
    const int idx = i * NTHR + tid, row = idx / (RROWS / 8), col = idx % (RROWS / 8);
    st[i] = ld_c(e + (size_t)row * N + nr * RROWS + col * 8);
  }
  __builtin_amdgcn_sched_barrier(0);
  float acc[4] = {0.f, 0.f, 0.f, 0.f};
#pragma unroll
  for (int t = 0; t < TILES; ++t) {
    float x[16];
#pragma unroll
    for (int v = 0; v < 16; ++v) {
      float d = 0.f;
      const u32x4 kk = kv[v % DEPTH];
      d = dot2(kk[0], qv[0], d); d = dot2(kk[1], qv[1], d); d = dot2(kk[2], qv[2], d); d = dot2(kk[3], qv[3], d);
      asm volatile("" : "+v"(d));
      x[v] = d;
      if (t * 16 + v + DEPTH < TILES * 16) kv[v % DEPTH] = ld_nt(kp + (size_t)(t * TROWS + 4 * (v + DEPTH)) * DH);
      __builtin_amdgcn_sched_barrier(0);
    }
    if (t == 0) {
#pragma unroll
      for (int i = 0; i < STAGE_PER_THR; ++i) {
        const int idx = i * NTHR + tid, row = idx / (RROWS / 8), col = idx % (RROWS / 8);
        *reinterpret_cast<u32x4*>(e_lds + row * PSTRIDE + col * 16) = st[i];
      }
    }
#define TR_STEP(HALF, CTRL, BIT) _Pragma("unroll") for (int hh = 0; hh < (HALF); ++hh) { \
      const float lo_ = x[hh], hi_ = x[hh + (HALF)]; const float t1_ = lo_ + dpp<CTRL>(lo_), t2_ = hi_ + dpp<CTRL>(hi_); \
      x[hh] = (c & (BIT)) ? t2_ : t1_; }
    TR_STEP(8, XOR8, 8) TR_STEP(4, XOR7, 4) TR_STEP(2, XOR2, 2) TR_STEP(1, XOR1, 1)
#undef TR_STEP
    s_lds[wave][4 * c + g] = x[0];
    if (t == 0) __syncthreads(); else __builtin_amdgcn_wave_barrier();
    f32x4 sv[4];
#pragma unroll
    for (int tt = 0; tt < 2; ++tt) {
      sv[tt * 2 + 0] = *reinterpret_cast<const f32x4*>(&s_lds[wave][32 * tt + 8 * g]);
      sv[tt * 2 + 1] = *reinterpret_cast<const f32x4*>(&s_lds[wave][32 * tt + 8 * g + 4]);
    }
#pragma unroll
    for (int i = 0; i < 4; ++i) {
      float a = acc[i];
#pragma unroll
      for (int tt = 0; tt < 2; ++tt) {
        const u32x4 ee = *reinterpret_cast<const u32x4*>(e_lds + (c + 16 * i) * PSTRIDE + (t * TROWS + 32 * tt + 8 * g) * 2);
        a = fmaf(bf_lo(ee[0]), sv[tt * 2][0], a); a = fmaf(bf_hi(ee[0]), sv[tt * 2][1], a);
        a = fmaf(bf_lo(ee[1]), sv[tt * 2][2], a); a = fmaf(bf_hi(ee[1]), sv[tt * 2][3], a);
        a = fmaf(bf_lo(ee[2]), sv[tt * 2 + 1][0], a); a = fmaf(bf_hi(ee[2]), sv[tt * 2 + 1][1], a);
        a = fmaf(bf_lo(ee[3]), sv[tt * 2 + 1][2], a); a = fmaf(bf_hi(ee[3]), sv[tt * 2 + 1][3], a);
      }
      acc[i] = a;
    }
    __builtin_amdgcn_wave_barrier();
  }
  {
    const float u0 = swap32_sum(acc[0], acc[2]);
    const float u1 = swap32_sum(acc[1], acc[3]);
    const float z = swap16_sum(u0, u1);
    const int i = 2 * (lane >> 5) + ((lane >> 4) & 1);
    part_sc[((size_t)h * NR + nr) * RK + c + 16 * i] = z;
  }
}

__global__ void __launch_bounds__(NTHR) k2_out(const u16* __restrict__ v, const u16* __restrict__ f,
                                               const float* __restrict__ part_sc, float* __restrict__ part_o,
                                               float scale) {
  constexpr int N = NSEQ;
  __shared__ __attribute__((aligned(16))) unsigned char f_lds[RK * PSTRIDE];
  __shared__ __attribute__((aligned(16))) float pw_lds[WAVES][TROWS];
  const int nr = blockIdx.x;
  const int tid = threadIdx.x, lane = tid & 63, wave = __builtin_amdgcn_readfirstlane(tid >> 6);
  const int h = blockIdx.y * WAVES + wave;
  const int g = lane >> 4, c = lane & 15;
  const int rg = lane >> 3, ng = lane & 7;
  const u16* vp = v + ((size_t)h * N + nr * RROWS + g) * DH + c * 8;
  float scp[NR];
#pragma unroll
  for (int i = 0; i < NR; ++i) scp[i] = part_sc[((size_t)h * NR + i) * RK + lane];
  __builtin_amdgcn_sched_barrier(0);
  u32x4 st[STAGE_PER_THR];
#pragma unroll
  for (int i = 0; i < STAGE_PER_THR; ++i) {
    const int idx = i * NTHR + tid, row = idx / (RROWS / 8), col = idx % (RROWS / 8);
    st[i] = ld_c(f + (size_t)row * N + nr * RROWS + col * 8);
  }
  __builtin_amdgcn_sched_barrier(0);
  u32x4 vv[DEPTH];
#pragma unroll
  for (int i = 0; i < DEPTH; ++i) vv[i] = ld_nt(vp + (size_t)(4 * i) * DH);
  __builtin_amdgcn_sched_barrier(0);
  float sc = 0.f;
#pragma unroll
  for (int i = 0; i < NR; ++i) sc += scp[i];
  sc *= scale;
  float m = sc;
#pragma unroll
  for (int o = 32; o >= 1; o >>= 1) m = fmaxf(m, __shfl_xor(m, o));
  const float ex = __expf(sc - m);
  float sum = ex;
#pragma unroll
  for (int o = 32; o >= 1; o >>= 1) sum += __shfl_xor(sum, o);
  pw_lds[wave][lane] = ex / sum;
#pragma unroll
  for (int i = 0; i < STAGE_PER_THR; ++i) {
    const int idx = i * NTHR + tid, row = idx / (RROWS / 8), col = idx % (RROWS / 8);
    *reinterpret_cast<u32x4*>(f_lds + row * PSTRIDE + col * 16) = st[i];
  }
  __syncthreads();
  float p[8];
  {
    const f32x4 a = *reinterpret_cast<const f32x4*>(&pw_lds[wave][8 * rg]);
    const f32x4 b = *reinterpret_cast<const f32x4*>(&pw_lds[wave][8 * rg + 4]);
    p[0] = a[0]; p[1] = a[1]; p[2] = a[2]; p[3] = a[3]; p[4] = b[0]; p[5] = b[1]; p[6] = b[2]; p[7] = b[3];
  }
  __builtin_amdgcn_wave_barrier();
  float acc[8];
#pragma unroll
  for (int j = 0; j < 8; ++j) acc[j] = 0.f;
#pragma unroll
  for (int t = 0; t < TILES; ++t) {
    float wacc[8];
#pragma unroll
    for (int j = 0; j < 8; ++j) wacc[j] = 0.f;
#pragma unroll
    for (int i = 0; i < 8; ++i) {
      const u32x4 ff = *reinterpret_cast<const u32x4*>(f_lds + (8 * rg + i) * PSTRIDE + (t * TROWS + 8 * ng) * 2);
      wacc[0] = fmaf(p[i], bf_lo(ff[0]), wacc[0]); wacc[1] = fmaf(p[i], bf_hi(ff[0]), wacc[1]);
      wacc[2] = fmaf(p[i], bf_lo(ff[1]), wacc[2]); wacc[3] = fmaf(p[i], bf_hi(ff[1]), wacc[3]);
      wacc[4] = fmaf(p[i], bf_lo(ff[2]), wacc[4]); wacc[5] = fmaf(p[i], bf_hi(ff[2]), wacc[5]);
      wacc[6] = fmaf(p[i], bf_lo(ff[3]), wacc[6]); wacc[7] = fmaf(p[i], bf_hi(ff[3]), wacc[7]);
    }
    const float u0 = swap32_sum(wacc[0], wacc[4]), u1 = swap32_sum(wacc[1], wacc[5]);
    const float u2 = swap32_sum(wacc[2], wacc[6]), u3 = swap32_sum(wacc[3], wacc[7]);
    const float t0 = swap16_sum(u0, u2), t1 = swap16_sum(u1, u3);
    const float lo = t0 + dpp<XOR8>(t0), hi = t1 + dpp<XOR8>(t1);
    const float wv = (lane & 8) ? hi : lo;
    {
      const int nl = 8 * ng + rg;
      pw_lds[wave][(nl & 3) * 16 + (nl >> 2)] = wv;
    }
    __builtin_amdgcn_wave_barrier();
    f32x4 w4[4];
#pragma unroll
    for (int i = 0; i < 4; ++i) w4[i] = *reinterpret_cast<const f32x4*>(&pw_lds[wave][g * 16 + 4 * i]);
    __builtin_amdgcn_sched_barrier(0);
#pragma unroll
    for (int i = 0; i < 16; ++i) {
      const u32x4 x = vv[i % DEPTH];
      const float w = w4[i >> 2][i & 3];
      acc[0] = fmaf(w, bf_lo(x[0]), acc[0]); acc[1] = fmaf(w, bf_hi(x[0]), acc[1]);
      acc[2] = fmaf(w, bf_lo(x[1]), acc[2]); acc[3] = fmaf(w, bf_hi(x[1]), acc[3]);
      acc[4] = fmaf(w, bf_lo(x[2]), acc[4]); acc[5] = fmaf(w, bf_hi(x[2]), acc[5]);
      acc[6] = fmaf(w, bf_lo(x[3]), acc[6]); acc[7] = fmaf(w, bf_hi(x[3]), acc[7]);
      asm volatile("" : "+v"(acc[0]), "+v"(acc[1]), "+v"(acc[2]), "+v"(acc[3]), "+v"(acc[4]), "+v"(acc[5]), "+v"(acc[6]), "+v"(acc[7]));
      if (t * 16 + i + DEPTH < TILES * 16) vv[i % DEPTH] = ld_nt(vp + (size_t)(t * TROWS + 4 * (i + DEPTH)) * DH);
      __builtin_amdgcn_sched_barrier(0);
    }
  }
  {
    const float u0 = swap32_sum(acc[0], acc[4]), u1 = swap32_sum(acc[1], acc[5]);
    const float u2 = swap32_sum(acc[2], acc[6]), u3 = swap32_sum(acc[3], acc[7]);
    const float t0 = swap16_sum(u0, u2), t1 = swap16_sum(u1, u3);
    const int jb = 4 * (lane >> 5) + 2 * ((lane >> 4) & 1);
    *reinterpret_cast<f32x2*>(part_o + ((size_t)h * NR + nr) * DH + c * 8 + jb) = f32x2{t0, t1};
  }
}

__global__ void __launch_bounds__(128) k3_final(const float* __restrict__ part_o, __hip_bfloat16* __restrict__ out) {
  const int h = blockIdx.x, d = threadIdx.x;
  float t = 0.f;
#pragma unroll
  for (int i = 0; i < NR; ++i) t += part_o[((size_t)h * NR + i) * DH + d];
  out[(size_t)h * DH + d] = __float2bfloat16(t);
}

extern "C" void kernel_launch(void* const* d_in, const int* in_sizes, int n_in,
                              void* d_out, int out_size, void* d_ws, size_t ws_size,
                              hipStream_t stream) {
  const u16* q = (const u16*)d_in[0];
  const u16* k = (const u16*)d_in[1];
  const u16* v = (const u16*)d_in[2];
  const u16* e = (const u16*)d_in[3];
  const u16* f = (const u16*)d_in[4];
  float* part_sc = (float*)d_ws;
  float* part_o = (float*)((char*)d_ws + (size_t)NBH * NR * RK * 4);
  const float scale = 1.0f / sqrtf((float)DH);
  dim3 grid(NR, NBH / WAVES);
  k1_scores<<<grid, NTHR, 0, stream>>>(q, k, e, part_sc);
  k2_out<<<grid, NTHR, 0, stream>>>(v, f, part_sc, part_o, scale);
  k3_final<<<NBH, 128, 0, stream>>>(part_o, (__hip_bfloat16*)d_out);
}
```

```cpp
#include <hip/hip_runtime.h>
#include <hip/hip_bf16.h>
#include <stdint.h>

typedef unsigned int u32;
typedef unsigned short u16;
typedef __attribute__((ext_vector_type(4))) unsigned int u32x4;
typedef __attribute__((ext_vector_type(4))) float f32x4;
typedef __attribute__((ext_vector_type(2))) float f32x2;
typedef __attribute__((ext_vector_type(2))) __bf16 bf16x2;

#define WAVES 4
#define TILES 4
#define DEPTH 8
#define DEPTH2 8
constexpr int DH = 128;
constexpr int RK = 64;
constexpr int TROWS = 64;
constexpr int NSEQ = 8192;
constexpr int NBH = 64;
constexpr int RROWS = TILES * TROWS;
constexpr int NR = NSEQ / RROWS;
constexpr int NTHR = WAVES * 64;
constexpr int PSTRIDE = RROWS * 2 + 16;
constexpr int STAGE_VEC = RK * RROWS * 2 / 16;
constexpr int STAGE_PER_THR = STAGE_VEC / NTHR;
static_assert(STAGE_VEC % NTHR == 0, "staging");

__device__ __forceinline__ float bf_lo(u32 x) { return __builtin_bit_cast(float, x << 16); }
__device__ __forceinline__ float bf_hi(u32 x) { return __builtin_bit_cast(float, x & 0xffff0000u); }
__device__ __forceinline__ float dot2(u32 a, u32 b, float acc) {
  return __builtin_amdgcn_fdot2_f32_bf16(__builtin_bit_cast(bf16x2, a), __builtin_bit_cast(bf16x2, b), acc, false);
}
__device__ __forceinline__ u32x4 ld_nt(const u16* p) { return __builtin_nontemporal_load(reinterpret_cast<const u32x4*>(p)); }
__device__ __forceinline__ u32x4 ld_c(const u16* p) { return *reinterpret_cast<const u32x4*>(p); }
template <int CTRL> __device__ __forceinline__ float dpp(float x) {
  return __builtin_bit_cast(float, __builtin_amdgcn_mov_dpp(__builtin_bit_cast(int, x), CTRL, 0xf, 0xf, true));
}
constexpr int XOR1 = 0xB1, XOR2 = 0x4E, XOR7 = 0x141, XOR8 = 0x128;
__device__ __forceinline__ float swap32_sum(float a, float b) {
  auto r = __builtin_amdgcn_permlane32_swap(__float_as_uint(a), __float_as_uint(b), false, false);
  return __uint_as_float(r[0]) + __uint_as_float(r[1]);
}
__device__ __forceinline__ float swap16_sum(float a, float b) {
  auto r = __builtin_amdgcn_permlane16_swap(__float_as_uint(a), __float_as_uint(b), false, false);
  return __uint_as_float(r[0]) + __uint_as_float(r[1]);
}

__global__ void __launch_bounds__(NTHR) k1_scores(const u16* __restrict__ q, const u16* __restrict__ k,
                                                  const u16* __restrict__ e, float* __restrict__ part_sc) {
  constexpr int N = NSEQ;
  __shared__ __attribute__((aligned(16))) unsigned char e_lds[RK * PSTRIDE];
  __shared__ __attribute__((aligned(16))) float s_lds[WAVES][TROWS];
  const int nr = blockIdx.x;
  const int tid = threadIdx.x, lane = tid & 63, wave = __builtin_amdgcn_readfirstlane(tid >> 6);
  const int h = blockIdx.y * WAVES + wave;
  const int g = lane >> 4, c = lane & 15;
  const u16* kp = k + ((size_t)h * N + nr * RROWS + g) * DH + c * 8;
  u32x4 st[STAGE_PER_THR];
#pragma unroll
  for (int i = 0; i < STAGE_PER_THR; ++i) {
    const int idx = i * NTHR + tid, row = idx / (RROWS / 8), col = idx % (RROWS / 8);
    st[i] = ld_c(e + (size_t)row * N + nr * RROWS + col * 8);
  }
  const u32x4 qv = ld_c(q + (size_t)h * DH + c * 8);
  __builtin_amdgcn_sched_barrier(0);
  u32x4 kv[DEPTH];
#pragma unroll
  for (int v = 0; v < DEPTH; ++v) kv[v] = ld_nt(kp + (size_t)(4 * v) * DH);
  __builtin_amdgcn_sched_barrier(0);
  float acc[4] = {0.f, 0.f, 0.f, 0.f};
  float xp[16];
  f32x4 sv[4];
  auto k1_piece = [&](int piece, int u) {
    if (piece == 0) {
#pragma unroll
      for (int hh = 0; hh < 8; ++hh) { const float lo_ = xp[hh], hi_ = xp[hh + 8];
        const float t1_ = lo_ + dpp<XOR8>(lo_), t2_ = hi_ + dpp<XOR8>(hi_); xp[hh] = (c & 8) ? t2_ : t1_; }
    } else if (piece == 1) {
#pragma unroll
      for (int hh = 0; hh < 4; ++hh) { const float lo_ = xp[hh], hi_ = xp[hh + 4];
        const float t1_ = lo_ + dpp<XOR7>(lo_), t2_ = hi_ + dpp<XOR7>(hi_); xp[hh] = (c & 4) ? t2_ : t1_; }
    } else if (piece == 2) {
#pragma unroll
      for (int hh = 0; hh < 2; ++hh) { const float lo_ = xp[hh], hi_ = xp[hh + 2];
        const float t1_ = lo_ + dpp<XOR2>(lo_), t2_ = hi_ + dpp<XOR2>(hi_); xp[hh] = (c & 2) ? t2_ : t1_; }
      { const float lo_ = xp[0], hi_ = xp[1];
        const float t1_ = lo_ + dpp<XOR1>(lo_), t2_ = hi_ + dpp<XOR1>(hi_); xp[0] = (c & 1) ? t2_ : t1_; }
      s_lds[wave][4 * c + g] = xp[0];
      __builtin_amdgcn_wave_barrier();
    } else if (piece == 3) {
#pragma unroll
      for (int tt = 0; tt < 2; ++tt) {
        sv[tt * 2 + 0] = *reinterpret_cast<const f32x4*>(&s_lds[wave][32 * tt + 8 * g]);
        sv[tt * 2 + 1] = *reinterpret_cast<const f32x4*>(&s_lds[wave][32 * tt + 8 * g + 4]);
      }
      __builtin_amdgcn_wave_barrier();
    } else if (piece >= 4 && piece < 12) {
      const int i = (piece - 4) >> 1, tt = (piece - 4) & 1;
      const u32x4 ee = *reinterpret_cast<const u32x4*>(e_lds + (c + 16 * i) * PSTRIDE + (u * TROWS + 32 * tt + 8 * g) * 2);
      float a = acc[i];
      a = fmaf(bf_lo(ee[0]), sv[tt * 2][0], a); a = fmaf(bf_hi(ee[0]), sv[tt * 2][1], a);
      a = fmaf(bf_lo(ee[1]), sv[tt * 2][2], a); a = fmaf(bf_hi(ee[1]), sv[tt * 2][3], a);
      a = fmaf(bf_lo(ee[2]), sv[tt * 2 + 1][0], a); a = fmaf(bf_hi(ee[2]), sv[tt * 2 + 1][1], a);
      a = fmaf(bf_lo(ee[3]), sv[tt * 2 + 1][2], a); a = fmaf(bf_hi(ee[3]), sv[tt * 2 + 1][3], a);
      asm volatile("" : "+v"(a));
      acc[i] = a;
    }
  };
#pragma unroll
  for (int t = 0; t < TILES; ++t) {
    float x[16];
#pragma unroll
    for (int v = 0; v < 16; ++v) {
      float d = 0.f;
      const u32x4 kk = kv[v % DEPTH];
      d = dot2(kk[0], qv[0], d); d = dot2(kk[1], qv[1], d); d = dot2(kk[2], qv[2], d); d = dot2(kk[3], qv[3], d);
      asm volatile("" : "+v"(d));
      x[v] = d;
      if (t * 16 + v + DEPTH < TILES * 16) kv[v % DEPTH] = ld_nt(kp + (size_t)(t * TROWS + 4 * (v + DEPTH)) * DH);
      if (t > 0) k1_piece(v, t - 1);
      __builtin_amdgcn_sched_barrier(0);
    }
#pragma unroll
    for (int v = 0; v < 16; ++v) xp[v] = x[v];
    if (t == 0) {
#pragma unroll
      for (int i = 0; i < STAGE_PER_THR; ++i) {
        const int idx = i * NTHR + tid, row = idx / (RROWS / 8), col = idx % (RROWS / 8);
        *reinterpret_cast<u32x4*>(e_lds + row * PSTRIDE + col * 16) = st[i];
      }
      __syncthreads();
    }
  }
#pragma unroll
  for (int piece = 0; piece < 12; ++piece) k1_piece(piece, TILES - 1);
  {
    const float u0 = swap32_sum(acc[0], acc[2]);
    const float u1 = swap32_sum(acc[1], acc[3]);
    const float z = swap16_sum(u0, u1);
    const int i = 2 * (lane >> 5) + ((lane >> 4) & 1);
    part_sc[((size_t)h * NR + nr) * RK + c + 16 * i] = z;
  }
}

__global__ void __launch_bounds__(NTHR) k2_out(const u16* __restrict__ v, const u16* __restrict__ f,
                                               const float* __restrict__ part_sc, float* __restrict__ part_o,
                                               float scale) {
  constexpr int N = NSEQ;
  __shared__ __attribute__((aligned(16))) unsigned char f_lds[RK * PSTRIDE];
  __shared__ __attribute__((aligned(16))) float pw_lds[WAVES][TROWS];
  const int nr = blockIdx.x;
  const int tid = threadIdx.x, lane = tid & 63, wave = __builtin_amdgcn_readfirstlane(tid >> 6);
  const int h = blockIdx.y * WAVES + wave;
  const int g = lane >> 4, c = lane & 15;
  const int rg = lane >> 3, ng = lane & 7;
  const u16* vp = v + ((size_t)h * N + nr * RROWS + g) * DH + c * 8;
  float scp[NR];
#pragma unroll
  for (int i = 0; i < NR; ++i) scp[i] = part_sc[((size_t)h * NR + i) * RK + lane];
  __builtin_amdgcn_sched_barrier(0);
  u32x4 st[STAGE_PER_THR];
#pragma unroll
  for (int i = 0; i < STAGE_PER_THR; ++i) {
    const int idx = i * NTHR + tid, row = idx / (RROWS / 8), col = idx % (RROWS / 8);
    st[i] = ld_c(f + (size_t)row * N + nr * RROWS + col * 8);
  }
  __builtin_amdgcn_sched_barrier(0);
  u32x4 vv[DEPTH2];
#pragma unroll
  for (int i = 0; i < DEPTH2; ++i) vv[i] = ld_nt(vp + (size_t)(4 * i) * DH);
  __builtin_amdgcn_sched_barrier(0);
  float sc = 0.f;
#pragma unroll
  for (int i = 0; i < NR; ++i) sc += scp[i];
  sc *= scale;
  float m = sc;
#pragma unroll
  for (int o = 32; o >= 1; o >>= 1) m = fmaxf(m, __shfl_xor(m, o));
  const float ex = __expf(sc - m);
  float sum = ex;
#pragma unroll
  for (int o = 32; o >= 1; o >>= 1) sum += __shfl_xor(sum, o);
  pw_lds[wave][lane] = ex / sum;
#pragma unroll
  for (int i = 0; i < STAGE_PER_THR; ++i) {
    const int idx = i * NTHR + tid, row = idx / (RROWS / 8), col = idx % (RROWS / 8);
    *reinterpret_cast<u32x4*>(f_lds + row * PSTRIDE + col * 16) = st[i];
  }
  __syncthreads();
  float p[8];
  {
    const f32x4 a = *reinterpret_cast<const f32x4*>(&pw_lds[wave][8 * rg]);
    const f32x4 b = *reinterpret_cast<const f32x4*>(&pw_lds[wave][8 * rg + 4]);
    p[0] = a[0]; p[1] = a[1]; p[2] = a[2]; p[3] = a[3]; p[4] = b[0]; p[5] = b[1]; p[6] = b[2]; p[7] = b[3];
  }
  __builtin_amdgcn_wave_barrier();
  float acc[8];
#pragma unroll
  for (int j = 0; j < 8; ++j) acc[j] = 0.f;
  float wacc[8];
  f32x4 w4[4], w4n[4];
  auto k2_piece = [&](int piece, int u, f32x4 (&dst)[4]) {
    if (piece < 8) {
      const int i = piece;
      if (i == 0) {
#pragma unroll
        for (int j = 0; j < 8; ++j) wacc[j] = 0.f;
      }
      const u32x4 ff = *reinterpret_cast<const u32x4*>(f_lds + (8 * rg + i) * PSTRIDE + (u * TROWS + 8 * ng) * 2);
      wacc[0] = fmaf(p[i], bf_lo(ff[0]), wacc[0]); wacc[1] = fmaf(p[i], bf_hi(ff[0]), wacc[1]);
      wacc[2] = fmaf(p[i], bf_lo(ff[1]), wacc[2]); wacc[3] = fmaf(p[i], bf_hi(ff[1]), wacc[3]);
      wacc[4] = fmaf(p[i], bf_lo(ff[2]), wacc[4]); wacc[5] = fmaf(p[i], bf_hi(ff[2]), wacc[5]);
      wacc[6] = fmaf(p[i], bf_lo(ff[3]), wacc[6]); wacc[7] = fmaf(p[i], bf_hi(ff[3]), wacc[7]);
      asm volatile("" : "+v"(wacc[0]), "+v"(wacc[1]), "+v"(wacc[2]), "+v"(wacc[3]), "+v"(wacc[4]), "+v"(wacc[5]), "+v"(wacc[6]), "+v"(wacc[7]));
    } else if (piece == 8) {
      wacc[0] = swap32_sum(wacc[0], wacc[4]); wacc[1] = swap32_sum(wacc[1], wacc[5]);
      wacc[2] = swap32_sum(wacc[2], wacc[6]); wacc[3] = swap32_sum(wacc[3], wacc[7]);
    } else if (piece == 9) {
      const float t0 = swap16_sum(wacc[0], wacc[2]), t1 = swap16_sum(wacc[1], wacc[3]);
      const float lo = t0 + dpp<XOR8>(t0), hi = t1 + dpp<XOR8>(t1);
      const float wv = (lane & 8) ? hi : lo;
      const int nl = 8 * ng + rg;
      pw_lds[wave][(nl & 3) * 16 + (nl >> 2)] = wv;
      __builtin_amdgcn_wave_barrier();
    } else if (piece == 10) {
#pragma unroll
      for (int i = 0; i < 4; ++i) dst[i] = *reinterpret_cast<const f32x4*>(&pw_lds[wave][g * 16 + 4 * i]);
      __builtin_amdgcn_wave_barrier();
    }
  };
#pragma unroll
  for (int piece = 0; piece < 11; ++piece) k2_piece(piece, 0, w4);
  __builtin_amdgcn_sched_barrier(0);
#pragma unroll
  for (int t = 0; t < TILES; ++t) {
#pragma unroll
    for (int i = 0; i < 16; ++i) {
      const u32x4 x = vv[i % DEPTH2];
      const float w = w4[i >> 2][i & 3];
      acc[0] = fmaf(w, bf_lo(x[0]), acc[0]); acc[1] = fmaf(w, bf_hi(x[0]), acc[1]);
      acc[2] = fmaf(w, bf_lo(x[1]), acc[2]); acc[3] = fmaf(w, bf_hi(x[1]), acc[3]);
      acc[4] = fmaf(w, bf_lo(x[2]), acc[4]); acc[5] = fmaf(w, bf_hi(x[2]), acc[5]);
      acc[6] = fmaf(w, bf_lo(x[3]), acc[6]); acc[7] = fmaf(w, bf_hi(x[3]), acc[7]);
      asm volatile("" : "+v"(acc[0]), "+v"(acc[1]), "+v"(acc[2]), "+v"(acc[3]), "+v"(acc[4]), "+v"(acc[5]), "+v"(acc[6]), "+v"(acc[7]));
      if (t * 16 + i + DEPTH2 < TILES * 16) vv[i % DEPTH2] = ld_nt(vp + (size_t)(t * TROWS + 4 * (i + DEPTH2)) * DH);
      if (t + 1 < TILES) k2_piece(i, t + 1, w4n);
      __builtin_amdgcn_sched_barrier(0);
    }
#pragma unroll
    for (int i = 0; i < 4; ++i) w4[i] = w4n[i];
  }
  {
    const float u0 = swap32_sum(acc[0], acc[4]), u1 = swap32_sum(acc[1], acc[5]);
    const float u2 = swap32_sum(acc[2], acc[6]), u3 = swap32_sum(acc[3], acc[7]);
    const float t0 = swap16_sum(u0, u2), t1 = swap16_sum(u1, u3);
    const int jb = 4 * (lane >> 5) + 2 * ((lane >> 4) & 1);
    *reinterpret_cast<f32x2*>(part_o + ((size_t)h * NR + nr) * DH + c * 8 + jb) = f32x2{t0, t1};
  }
}

__global__ void __launch_bounds__(128) k3_final(const float* __restrict__ part_o, __hip_bfloat16* __restrict__ out) {
  const int h = blockIdx.x, d = threadIdx.x;
  float t = 0.f;
#pragma unroll
  for (int i = 0; i < NR; ++i) t += part_o[((size_t)h * NR + i) * DH + d];
  out[(size_t)h * DH + d] = __float2bfloat16(t);
}

extern "C" void kernel_launch(void* const* d_in, const int* in_sizes, int n_in,
                              void* d_out, int out_size, void* d_ws, size_t ws_size,
                              hipStream_t stream) {
  const u16* q = (const u16*)d_in[0];
  const u16* k = (const u16*)d_in[1];
  const u16* v = (const u16*)d_in[2];
  const u16* e = (const u16*)d_in[3];
  const u16* f = (const u16*)d_in[4];
  float* part_sc = (float*)d_ws;
  float* part_o = (float*)((char*)d_ws + (size_t)NBH * NR * RK * 4);
  const float scale = 1.0f / sqrtf((float)DH);
  dim3 grid(NR, NBH / WAVES);
  k1_scores<<<grid, NTHR, 0, stream>>>(q, k, e, part_sc);
  k2_out<<<grid, NTHR, 0, stream>>>(v, f, part_sc, part_o, scale);
  k3_final<<<NBH, 128, 0, stream>>>(part_o, (__hip_bfloat16*)d_out);
}
```
